```python
import torch

def _softmax(x, axis=-1):
    return torch.softmax(x, dim=axis)
def _gelu(x):
    return torch.nn.functional.gelu(x, approximate="tanh")
def _silu(x):
    return torch.nn.functional.silu(x)
def _sigmoid(x):
    return torch.sigmoid(x)
def _layernorm(x, axis=-1, eps=1e-5):
    m = x.mean(dim=axis, keepdim=True)
    v = x.var(dim=axis, keepdim=True, unbiased=False)
    return (x - m) / torch.sqrt(v + eps)
def _scatter_add(tgt, idx, src, axis=0):
    return tgt.clone().index_add_(axis, idx, src)
def _segment_sum(src, seg, n):
    out = torch.zeros((n,) + src.shape[1:], dtype=src.dtype)
    return out.index_add_(0, seg, src)
def _pad(x, pw):
    flat = tuple(v for pair in reversed(pw) for v in pair)
    return torch.nn.functional.pad(x, flat)

kernel_name = 'standalone__softmax_full_bidir__decode'

def setup_inputs():

    _w = (lambda a: (lambda t: t.to(torch.bfloat16) if t.is_floating_point() else t)(torch.as_tensor(a)))
    B, H, Sq, Skv, Dh = 4, 32, 1, 4096, 192
    return {
        "q": _w((lambda *s: torch.randn(*s, dtype=torch.bfloat16) * 0.8)(B, H, Sq, Dh)),
        "k": _w((lambda *s: torch.randn(*s, dtype=torch.bfloat16) * 0.8)(B, H, Skv, Dh)),
        "v": _w((lambda *s: torch.randn(*s, dtype=torch.bfloat16) * 0.05)(B, H, Skv, Dh)),
    }

def reference(q, k, v):
    scale = q.shape[-1] ** -0.5
    scores = torch.einsum('bhqd,bhkd->bhqk', q, k) * scale
    attn = _softmax(scores, axis=-1)
    return torch.einsum('bhqk,bhkd->bhqd', attn, v)
```

```cpp
#include <hip/hip_runtime.h>
#include <hip/hip_bf16.h>
#include <stdint.h>
#pragma clang fp contract(off)

using u32x4 = __attribute__((ext_vector_type(4))) unsigned;
using u32x2 = __attribute__((ext_vector_type(2))) unsigned;
using f32x4 = __attribute__((ext_vector_type(4))) float;
using f32x2 = __attribute__((ext_vector_type(2))) float;
using bf16x2 = __attribute__((ext_vector_type(2))) __bf16;
typedef __attribute__((address_space(1))) unsigned gu32;
typedef __attribute__((address_space(3))) unsigned lu32;

constexpr int NH = 128, SKV = 4096, D = 192, ROWB = D * 2;
constexpr int HEADB = SKV * ROWB;
constexpr int WAVES = 4;
constexpr int TILE = 16;
constexpr int TILEB = TILE * ROWB;
constexpr int STEPB = WAVES * TILEB;
constexpr int NSTEP = HEADB / STEPB;
constexpr int NTH = 33, NTL = NSTEP - NTH;
constexpr int RING = 2;
constexpr int MIX = NTH % RING;
constexpr int NEAR_END = 8;
constexpr unsigned PACE = 1345;
constexpr int AUX_NT = 2, AUX_SC1 = 16;
static_assert(NTL % RING == MIX && (MIX == 0 || 2 * MIX == RING) && NTH >= NTL && NTL > 24 && NEAR_END < NTL - RING, "schedule");
constexpr float C2 = 0.07216878364870322f * 1.4426950408889634f;
constexpr int REC = 200;
constexpr int SCW = NTH * TILE;
constexpr unsigned L_NEAR = 0x100u, L_COUNT = 0xffu;
constexpr int WS_INBOX_B = 2048, WS_CNT_OFF = 1 << 20;
constexpr unsigned P_START = 1u, P_ACK = 4u, P_FIN = 16u;
constexpr unsigned TAG = 0x5EED0000u;

#define SB() __builtin_amdgcn_sched_barrier(0)

__device__ __forceinline__ float dot2(unsigned a, unsigned b, float c) {
  return __builtin_amdgcn_fdot2_f32_bf16(__builtin_bit_cast(bf16x2, a), __builtin_bit_cast(bf16x2, b), c, false);
}
template <int CTRL> __device__ __forceinline__ float dpp(float x) {
  return __builtin_bit_cast(float, __builtin_amdgcn_mov_dpp(__builtin_bit_cast(int, x), CTRL, 0xf, 0xf, true));
}
constexpr int XOR1 = 0xB1, XOR2 = 0x4E, XOR7 = 0x141, XOR8 = 0x128;
__device__ __forceinline__ float x16_32_sum(float x) {
  auto s = __builtin_amdgcn_permlane16_swap(__float_as_uint(x), __float_as_uint(x), false, false);
  x = __uint_as_float(s[0]) + __uint_as_float(s[1]);
  auto t = __builtin_amdgcn_permlane32_swap(__float_as_uint(x), __float_as_uint(x), false, false);
  return __uint_as_float(t[0]) + __uint_as_float(t[1]);
}
__device__ __forceinline__ float x16_32_max(float x) {
  auto s = __builtin_amdgcn_permlane16_swap(__float_as_uint(x), __float_as_uint(x), false, false);
  x = fmaxf(__uint_as_float(s[0]), __uint_as_float(s[1]));
  auto t = __builtin_amdgcn_permlane32_swap(__float_as_uint(x), __float_as_uint(x), false, false);
  return fmaxf(__uint_as_float(t[0]), __uint_as_float(t[1]));
}
__device__ __forceinline__ unsigned add_ret(gu32* p, unsigned x) {
  int z = 0; asm volatile("" : "+v"(z));
  return __hip_atomic_fetch_add(p + z, x, __ATOMIC_RELAXED, __HIP_MEMORY_SCOPE_AGENT);
}
__device__ __forceinline__ unsigned peek(gu32* p) {
  return __builtin_amdgcn_readfirstlane(__hip_atomic_load(p, __ATOMIC_RELAXED, __HIP_MEMORY_SCOPE_AGENT));
}
__device__ __forceinline__ unsigned lds_peek(lu32* p) {
  return __builtin_amdgcn_readfirstlane(__hip_atomic_load(p, __ATOMIC_RELAXED, __HIP_MEMORY_SCOPE_WORKGROUP));
}
__device__ __forceinline__ unsigned clock16() { return (unsigned)__builtin_amdgcn_s_memrealtime() << 4; }

struct Tile { u32x4 r[6]; };

__device__ __forceinline__ void load_tile(Tile& t, const char* kh, const char* vh, int light, int n, int rot, int pos, int voff) {
  const bool isv = pos >= n;
  int i = (isv ? pos - n : pos) + rot;
  i = i >= n ? i - n : i;
  const int step = i + min(i, NTL) + light;
  const __amdgpu_buffer_rsrc_t rs = __builtin_amdgcn_make_buffer_rsrc((void*)(isv ? vh : kh), 0, pos < 2 * n ? HEADB : 0, 0x00020000);
  const int so = step * STEPB;
  t.r[0] = __builtin_amdgcn_raw_buffer_load_b128(rs, voff, so, AUX_NT);
  t.r[1] = __builtin_amdgcn_raw_buffer_load_b128(rs, voff + 128, so, AUX_NT);
  t.r[2] = __builtin_amdgcn_raw_buffer_load_b128(rs, voff + 256, so, AUX_NT);
  t.r[3] = __builtin_amdgcn_raw_buffer_load_b128(rs, voff + 8 * ROWB, so, AUX_NT);
  t.r[4] = __builtin_amdgcn_raw_buffer_load_b128(rs, voff + 8 * ROWB + 128, so, AUX_NT);
  t.r[5] = __builtin_amdgcn_raw_buffer_load_b128(rs, voff + 8 * ROWB + 256, so, AUX_NT);
}

__device__ __forceinline__ void scores(const Tile& k, const u32x4 (&q)[3], float& s0, float& s1) {
  float a = 0.f, b = 0.f;
#pragma unroll
  for (int i = 0; i < 3; ++i)
#pragma unroll
    for (int j = 0; j < 4; ++j) { a = dot2(k.r[i][j], q[i][j], a); b = dot2(k.r[3 + i][j], q[i][j], b); }
  a += dpp<XOR1>(a); b += dpp<XOR1>(b);
  a += dpp<XOR2>(a); b += dpp<XOR2>(b);
  a += dpp<XOR7>(a); b += dpp<XOR7>(b);
  s0 = a * C2; s1 = b * C2;
}

__device__ __forceinline__ void pv(const Tile& v, f32x2 s, float M, float& l, float (&acc)[24]) {
  const float p0 = __builtin_amdgcn_exp2f(s[0] - M), p1 = __builtin_amdgcn_exp2f(s[1] - M);
  l += p0 + p1;
#pragma unroll
  for (int i = 0; i < 3; ++i)
#pragma unroll
    for (int j = 0; j < 4; ++j) {
      const unsigned u0 = v.r[i][j], u1 = v.r[3 + i][j];
      float lo = acc[i * 8 + 2 * j], hi = acc[i * 8 + 2 * j + 1];
      lo = fmaf(p0, __uint_as_float(u0 << 16), lo); hi = fmaf(p0, __uint_as_float(u0 & 0xffff0000u), hi);
      lo = fmaf(p1, __uint_as_float(u1 << 16), lo); hi = fmaf(p1, __uint_as_float(u1 & 0xffff0000u), hi);
      acc[i * 8 + 2 * j] = lo; acc[i * 8 + 2 * j + 1] = hi;
    }
}

__device__ __forceinline__ void wave_record(float* rec, int lane, float M, float l, float (&acc)[24]) {
  l += dpp<XOR8>(l); l = x16_32_sum(l);
  float x[12], y[6];
#pragma unroll
  for (int i = 0; i < 3; ++i)
#pragma unroll
    for (int e = 0; e < 4; ++e) {
      auto s = __builtin_amdgcn_permlane32_swap(__float_as_uint(acc[i * 8 + e]), __float_as_uint(acc[i * 8 + e + 4]), false, false);
      x[i * 4 + e] = __uint_as_float(s[0]) + __uint_as_float(s[1]);
    }
#pragma unroll
  for (int i = 0; i < 3; ++i)
#pragma unroll
    for (int e = 0; e < 2; ++e) {
      auto s = __builtin_amdgcn_permlane16_swap(__float_as_uint(x[i * 4 + e]), __float_as_uint(x[i * 4 + e + 2]), false, false);
      const float t = __uint_as_float(s[0]) + __uint_as_float(s[1]);
      y[i * 2 + e] = t + dpp<XOR8>(t);
    }
  if ((lane & 8) == 0) {
    float* p = rec + (lane & 7) * 8 + (lane >> 5) * 4 + ((lane >> 4) & 1) * 2;
#pragma unroll
    for (int i = 0; i < 3; ++i) *reinterpret_cast<f32x2*>(p + i * 64) = f32x2{y[i * 2], y[i * 2 + 1]};
    if (lane == 0) *reinterpret_cast<f32x2*>(rec + 192) = f32x2{M, l};
  }
}

__device__ __forceinline__ void part_merge(const float* lds, int lane, f32x4& o, float& MM, float& L) {
  const int dl = (lane < 48 ? lane : 0) * 4;
  f32x2 ml[WAVES]; f32x4 a[WAVES];
#pragma unroll
  for (int x = 0; x < WAVES; ++x) { ml[x] = *reinterpret_cast<const f32x2*>(lds + x * REC + 192); a[x] = *reinterpret_cast<const f32x4*>(lds + x * REC + dl); }
  SB();
  MM = fmaxf(fmaxf(ml[0][0], ml[1][0]), fmaxf(ml[2][0], ml[3][0]));
  L = 0.f;
  o = f32x4{0.f, 0.f, 0.f, 0.f};
#pragma unroll
  for (int x = 0; x < WAVES; ++x) {
    const float wt = __builtin_amdgcn_exp2f(ml[x][0] - MM);
    L = fmaf(wt, ml[x][1], L);
    o[0] = fmaf(wt, a[x][0], o[0]); o[1] = fmaf(wt, a[x][1], o[1]); o[2] = fmaf(wt, a[x][2], o[2]); o[3] = fmaf(wt, a[x][3], o[3]);
  }
}

#define PIN_MAX() asm volatile("" : "+v"(m) :: "memory")
#define PIN_ACC() asm volatile("" : "+v"(acc[0]), "+v"(acc[1]), "+v"(acc[2]), "+v"(acc[3]), "+v"(acc[4]), "+v"(acc[5]), "+v"(acc[6]), "+v"(acc[7]), \
  "+v"(acc[8]), "+v"(acc[9]), "+v"(acc[10]), "+v"(acc[11]), "+v"(acc[12]), "+v"(acc[13]), "+v"(acc[14]), "+v"(acc[15]), \
  "+v"(acc[16]), "+v"(acc[17]), "+v"(acc[18]), "+v"(acc[19]), "+v"(acc[20]), "+v"(acc[21]), "+v"(acc[22]), "+v"(acc[23]), "+v"(l) :: "memory")

template <bool MAIN>
__device__ __forceinline__ void stream_part(const char* kh, const char* vh, const uint16_t* qh, int light, int rot, int w, int lane, float* sc,
                                            float& M, float& l, float (&acc)[24], lu32* word) {
  const int n = light ? NTL : NTH;
  const int voff = w * TILEB + (lane >> 3) * ROWB + (lane & 7) * 16;
  f32x2* const scw = reinterpret_cast<f32x2*>(sc) + (lane >> 3);
  u32x4 qf[3];
#pragma unroll
  for (int i = 0; i < 3; ++i) qf[i] = *reinterpret_cast<const u32x4*>(qh + i * 64 + (lane & 7) * 8);
  Tile t[RING];
  SB();
#pragma unroll
  for (int d = 0; d < RING; ++d) { load_tile(t[d], kh, vh, light, n, rot, d, voff); SB(); }
  if (MAIN) { __builtin_amdgcn_s_barrier(); SB(); }
  unsigned tnext = MAIN ? clock16() - PACE : 0u;
  auto pace = [&](unsigned seen, int pos) {
    tnext += (pos < 2 * NTL + RING) ? PACE : PACE / 2;
    if ((int)(seen - tnext) < 0) { while ((int)(clock16() - tnext) < 0) __builtin_amdgcn_s_sleep(1); }
  };
  float m = -INFINITY;
  auto kstep = [&](Tile& tt, int pos) {
    float s0, s1;
    const unsigned now = MAIN ? clock16() : 0u;
    scores(tt, qf, s0, s1);
    m = fmaxf(m, fmaxf(s0, s1));
    if ((lane & 7) == 0) scw[pos * 8] = f32x2{s0, s1};
    PIN_MAX(); SB();
    if (MAIN) { pace(now, pos + RING); SB(); }
    load_tile(tt, kh, vh, light, n, rot, pos + RING, voff);
    SB();
  };
  auto vstep = [&](Tile& tt, int j) {
    const unsigned now = MAIN ? clock16() : 0u;
    pv(tt, scw[j * 8], M, l, acc);
    PIN_ACC(); SB();
    if (MAIN && n + j + RING < 2 * n) pace(now, n + j + RING);
    SB();
    load_tile(tt, kh, vh, light, n, rot, n + j + RING, voff);
    SB();
  };
  int p = 0;
  for (; p < n - MIX; p += RING) {
#pragma unroll
    for (int d = 0; d < RING; ++d) kstep(t[d], p + d);
  }
#pragma unroll
  for (int d = 0; d < MIX; ++d) kstep(t[d], p + d);
  M = fmaxf(m, dpp<XOR8>(m));
  M = fmaxf(x16_32_max(M), -3.0e38f);
  l = 0.f;
#pragma unroll
  for (int i = 0; i < 24; ++i) acc[i] = 0.f;
  SB();
#pragma unroll
  for (int d = MIX; d < (MIX ? RING : 0); ++d) vstep(t[d], d - MIX);
  for (int j = MIX ? RING - MIX : 0; j < n; j += RING) {
    if (MAIN && w == 0 && j + NEAR_END >= n && j + NEAR_END < n + RING) {
      if (lane == 0) __hip_atomic_fetch_or(word, L_NEAR, __ATOMIC_RELAXED, __HIP_MEMORY_SCOPE_WORKGROUP);
      SB();
    }
#pragma unroll
    for (int d = 0; d < RING; ++d) vstep(t[d], j + d);
  }
}

__global__ void __launch_bounds__((WAVES + 1) * 64, 2)
attn_decode(const uint16_t* __restrict__ q, const uint16_t* __restrict__ k, const uint16_t* __restrict__ v,
            __hip_bfloat16* out, char* ws) {
  __shared__ __attribute__((aligned(16))) float lds[WAVES * REC + 4 + WAVES * SCW];
  lu32* const arrived = (lu32*)(lds + WAVES * REC);
  float* const scores_lds = lds + WAVES * REC + 4;
  const int wg = blockIdx.x, head = wg >> 1;
  const int tid = threadIdx.x, lane = tid & 63, wave = __builtin_amdgcn_readfirstlane(tid >> 6);
  const int light = __builtin_amdgcn_s_getreg(20 | (3 << 11)) & 1;
  const char* kh = reinterpret_cast<const char*>(k) + (size_t)head * HEADB;
  const char* vh = reinterpret_cast<const char*>(v) + (size_t)head * HEADB;
  const uint16_t* qh = q + head * D;
  const int rot = (head & 3) * 8;
  float m, l, acc[24];

  if (__builtin_expect(wave < WAVES, 1)) {
    stream_part<true>(kh, vh, qh, light, rot, wave, lane, scores_lds + wave * SCW, m, l, acc, arrived);
    wave_record(lds + wave * REC, lane, m, l, acc);
    asm volatile("s_waitcnt lgkmcnt(0)" ::: "memory");
    if (lane == 0) __hip_atomic_fetch_add(arrived, 1u, __ATOMIC_RELAXED, __HIP_MEMORY_SCOPE_WORKGROUP);
    return;
  }

  if (lane == 0) __hip_atomic_store(arrived, 0u, __ATOMIC_RELAXED, __HIP_MEMORY_SCOPE_WORKGROUP);
  asm volatile("s_waitcnt lgkmcnt(0)" ::: "memory");
  SB();
  __builtin_amdgcn_s_barrier();
  SB();
  gu32* const cnt = (gu32*)(ws + WS_CNT_OFF + head * 256);
  const __amdgpu_buffer_rsrc_t mybox = __builtin_amdgcn_make_buffer_rsrc((void*)(ws + (size_t)wg * WS_INBOX_B), 0, WS_INBOX_B, 0x00020000);
  const int boff = lane <= 48 ? lane * 16 : 0x7ff0;
  __builtin_amdgcn_raw_buffer_store_b128(u32x4{0u, 0u, 0u, 0u}, mybox, boff, 0, AUX_SC1);
  __builtin_amdgcn_raw_buffer_store_b128(u32x4{0u, 0u, 0u, 0u}, mybox, boff + 1024, 0, AUX_SC1);
  asm volatile("s_waitcnt vmcnt(0)" ::: "memory");
  unsigned tick = 0;
  if (lane == 0) tick = add_ret(cnt, P_START);
  tick = __builtin_amdgcn_readfirstlane(tick) + P_START;
  bool known = false, have = false;
  u32x4 r0 = {0, 0, 0, 0}, r1 = {0, 0, 0, 0};
  auto meet = [&]() {
    const unsigned d = peek(cnt) - tick;
    if (d & P_START) {
      if (lane == 0) add_ret(cnt, P_ACK);
      asm volatile("s_waitcnt vmcnt(0)" ::: "memory");
    }
    known = (d & (P_START | P_ACK)) != 0;
  };
  auto look = [&]() {
    asm volatile("" ::: "memory");
    r0 = __builtin_amdgcn_raw_buffer_load_b128(mybox, boff, 0, AUX_SC1);
    r1 = __builtin_amdgcn_raw_buffer_load_b128(mybox, boff + 1024, 0, AUX_SC1);
    const bool ok = lane > 48 || (((r0[1] ^ TAG) | (r0[3] ^ TAG) | (r1[1] ^ TAG) | (r1[3] ^ TAG)) >> 1) == 0;
    have = __all(ok);
  };
  while (!(lds_peek(arrived) & L_NEAR)) {
    if (known) __builtin_amdgcn_s_sleep(32);
    else { __builtin_amdgcn_s_sleep(127); meet(); }
  }
  for (;;) {
    look();
    if (have || (lds_peek(arrived) & L_COUNT) == WAVES) break;
    if (!known) meet();
    __builtin_amdgcn_s_sleep(4);
  }
  while ((lds_peek(arrived) & L_COUNT) != WAVES) __builtin_amdgcn_s_sleep(1);
  asm volatile("" ::: "memory");

  f32x4 o; float MM, L;
  part_merge(lds, lane, o, MM, L);

  if (!have) {
    for (unsigned spins = 0; !known && spins < (1u << 22); ++spins) { meet(); __builtin_amdgcn_s_sleep(4); }
    const __amdgpu_buffer_rsrc_t peer = __builtin_amdgcn_make_buffer_rsrc((void*)(ws + (size_t)(wg ^ 1) * WS_INBOX_B), 0, known ? WS_INBOX_B : 0, 0x00020000);
    const unsigned tag = TAG | (unsigned)light;
    const float v0 = lane < 48 ? o[0] : MM, v1 = lane < 48 ? o[1] : L;
    __builtin_amdgcn_raw_buffer_store_b128(u32x4{__float_as_uint(v0), tag, __float_as_uint(v1), tag}, peer, boff, 0, AUX_SC1);
    __builtin_amdgcn_raw_buffer_store_b128(u32x4{__float_as_uint(o[2]), tag, __float_as_uint(o[3]), tag}, peer, boff + 1024, 0, AUX_SC1);
    unsigned e = 0;
    if (lane == 0) e = add_ret(cnt, P_FIN);
    look();
    if (!have && ((__builtin_amdgcn_readfirstlane(e) - tick) & P_FIN)) {
      for (unsigned spins = 0; !have && spins < (1u << 22); ++spins) look();
    }
  }
  if (have) {
    f32x4 p = {__uint_as_float(r0[0]), __uint_as_float(r0[2]), __uint_as_float(r1[0]), __uint_as_float(r1[2])};
    float pM = __uint_as_float(__builtin_amdgcn_readlane(r0[0], 48)), pL = __uint_as_float(__builtin_amdgcn_readlane(r0[2], 48));
    if ((int)(__builtin_amdgcn_readlane(r0[1], 48) & 1) == light) {
      for (int w = 0; w < WAVES; ++w) {
        stream_part<false>(kh, vh, qh, light ^ 1, rot, w, lane, scores_lds + w * SCW, m, l, acc, arrived);
        wave_record(lds + w * REC, lane, m, l, acc);
      }
      asm volatile("s_waitcnt lgkmcnt(0)" ::: "memory");
      part_merge(lds, lane, p, pM, pL);
    }
    const f32x4 oh = light ? p : o, ol = light ? o : p;
    const float Mh = light ? pM : MM, Ml = light ? MM : pM, Lh = light ? pL : L, Ll = light ? L : pL;
    const float Mx = fmaxf(Mh, Ml);
    const float wh = __builtin_amdgcn_exp2f(Mh - Mx), wl = __builtin_amdgcn_exp2f(Ml - Mx);
    const float inv = __builtin_amdgcn_rcpf(fmaf(wl, Ll, wh * Lh));
    const float a = wh * inv, b = wl * inv;
    if (lane < 48) {
      const bf16x2 x = {(__bf16)fmaf(b, ol[0], a * oh[0]), (__bf16)fmaf(b, ol[1], a * oh[1])};
      const bf16x2 y = {(__bf16)fmaf(b, ol[2], a * oh[2]), (__bf16)fmaf(b, ol[3], a * oh[3])};
      *reinterpret_cast<u32x2*>(reinterpret_cast<char*>(out) + (size_t)head * ROWB + lane * 8) =
          u32x2{__builtin_bit_cast(unsigned, x), __builtin_bit_cast(unsigned, y)};
    }
  }
}

extern "C" void kernel_launch(void* const* d_in, const int* in_sizes, int n_in,
                              void* d_out, int out_size, void* d_ws, size_t ws_size,
                              hipStream_t stream) {
  const uint16_t* q = (const uint16_t*)d_in[0];
  const uint16_t* k = (const uint16_t*)d_in[1];
  const uint16_t* v = (const uint16_t*)d_in[2];
  attn_decode<<<NH * 2, (WAVES + 1) * 64, 0, stream>>>(q, k, v, (__hip_bfloat16*)d_out, (char*)d_ws);
}
```

```cpp
#include <hip/hip_runtime.h>
#include <hip/hip_bf16.h>
#include <stdint.h>
#pragma clang fp contract(off)

using u32x4 = __attribute__((ext_vector_type(4))) unsigned;
using u32x2 = __attribute__((ext_vector_type(2))) unsigned;
using f32x4 = __attribute__((ext_vector_type(4))) float;
using f32x2 = __attribute__((ext_vector_type(2))) float;
using bf16x2 = __attribute__((ext_vector_type(2))) __bf16;
typedef __attribute__((address_space(1))) unsigned gu32;
typedef __attribute__((address_space(3))) unsigned lu32;

constexpr int NH = 128, SKV = 4096, D = 192, ROWB = D * 2;
constexpr int HEADB = SKV * ROWB;
constexpr int WAVES = 4;
constexpr int TILE = 16;
constexpr int TILEB = TILE * ROWB;
constexpr int STEPB = WAVES * TILEB;
constexpr int NSTEP = HEADB / STEPB;
constexpr int NTH = 33, NTL = NSTEP - NTH;
constexpr int RING = 2;
constexpr int MIX = NTH % RING;
constexpr int NEAR_END = 8;
constexpr unsigned PACE = 1350;
constexpr int AUX_NT = 2, AUX_SC1 = 16;
static_assert(NTL % RING == MIX && (MIX == 0 || 2 * MIX == RING) && NTH >= NTL && NTL > 24 && NEAR_END < NTL - RING, "schedule");
constexpr float C2 = 0.07216878364870322f * 1.4426950408889634f;
constexpr int REC = 200;
constexpr int SCW = NTH * TILE;
constexpr unsigned L_NEAR = 0x100u, L_COUNT = 0xffu;
constexpr int WS_INBOX_B = 2048, WS_CNT_OFF = 1 << 20;
constexpr unsigned P_START = 1u, P_ACK = 4u, P_FIN = 16u;
constexpr unsigned TAG = 0x5EED0000u;
constexpr unsigned SPIN_CAP = 1u << 20;

#define SB() __builtin_amdgcn_sched_barrier(0)

__device__ __forceinline__ float dot2(unsigned a, unsigned b, float c) {
  return __builtin_amdgcn_fdot2_f32_bf16(__builtin_bit_cast(bf16x2, a), __builtin_bit_cast(bf16x2, b), c, false);
}
template <int CTRL> __device__ __forceinline__ float dpp(float x) {
  return __builtin_bit_cast(float, __builtin_amdgcn_mov_dpp(__builtin_bit_cast(int, x), CTRL, 0xf, 0xf, true));
}
constexpr int XOR1 = 0xB1, XOR2 = 0x4E, XOR7 = 0x141, XOR8 = 0x128;
__device__ __forceinline__ float x16_32_sum(float x) {
  auto s = __builtin_amdgcn_permlane16_swap(__float_as_uint(x), __float_as_uint(x), false, false);
  x = __uint_as_float(s[0]) + __uint_as_float(s[1]);
  auto t = __builtin_amdgcn_permlane32_swap(__float_as_uint(x), __float_as_uint(x), false, false);
  return __uint_as_float(t[0]) + __uint_as_float(t[1]);
}
__device__ __forceinline__ float x16_32_max(float x) {
  auto s = __builtin_amdgcn_permlane16_swap(__float_as_uint(x), __float_as_uint(x), false, false);
  x = fmaxf(__uint_as_float(s[0]), __uint_as_float(s[1]));
  auto t = __builtin_amdgcn_permlane32_swap(__float_as_uint(x), __float_as_uint(x), false, false);
  return fmaxf(__uint_as_float(t[0]), __uint_as_float(t[1]));
}
__device__ __forceinline__ unsigned add_ret(gu32* p, unsigned x) {
  int z = 0; asm volatile("" : "+v"(z));
  return __hip_atomic_fetch_add(p + z, x, __ATOMIC_RELAXED, __HIP_MEMORY_SCOPE_AGENT);
}
__device__ __forceinline__ unsigned peek(gu32* p) {
  return __builtin_amdgcn_readfirstlane(__hip_atomic_load(p, __ATOMIC_RELAXED, __HIP_MEMORY_SCOPE_AGENT));
}
__device__ __forceinline__ unsigned lds_peek(lu32* p) {
  return __builtin_amdgcn_readfirstlane(__hip_atomic_load(p, __ATOMIC_RELAXED, __HIP_MEMORY_SCOPE_WORKGROUP));
}
__device__ __forceinline__ unsigned clock16() { return (unsigned)__builtin_amdgcn_s_memrealtime() << 4; }

struct Tile { u32x4 r[6]; };

__device__ __forceinline__ void load_tile(Tile& t, const char* kh, const char* vh, int light, int n, int rot, int pos, int voff) {
  const bool isv = pos >= n;
  int i = (isv ? pos - n : pos) + rot;
  i = i >= n ? i - n : i;
  const int step = i + min(i, NTL) + light;
  const __amdgpu_buffer_rsrc_t rs = __builtin_amdgcn_make_buffer_rsrc((void*)(isv ? vh : kh), 0, pos < 2 * n ? HEADB : 0, 0x00020000);
  const int so = step * STEPB;
  t.r[0] = __builtin_amdgcn_raw_buffer_load_b128(rs, voff, so, AUX_NT);
  t.r[1] = __builtin_amdgcn_raw_buffer_load_b128(rs, voff + 128, so, AUX_NT);
  t.r[2] = __builtin_amdgcn_raw_buffer_load_b128(rs, voff + 256, so, AUX_NT);
  t.r[3] = __builtin_amdgcn_raw_buffer_load_b128(rs, voff + 8 * ROWB, so, AUX_NT);
  t.r[4] = __builtin_amdgcn_raw_buffer_load_b128(rs, voff + 8 * ROWB + 128, so, AUX_NT);
  t.r[5] = __builtin_amdgcn_raw_buffer_load_b128(rs, voff + 8 * ROWB + 256, so, AUX_NT);
}

__device__ __forceinline__ void scores(const Tile& k, const u32x4 (&q)[3], float& s0, float& s1) {
  float a = 0.f, b = 0.f;
#pragma unroll
  for (int i = 0; i < 3; ++i)
#pragma unroll
    for (int j = 0; j < 4; ++j) { a = dot2(k.r[i][j], q[i][j], a); b = dot2(k.r[3 + i][j], q[i][j], b); }
  a += dpp<XOR1>(a); b += dpp<XOR1>(b);
  a += dpp<XOR2>(a); b += dpp<XOR2>(b);
  a += dpp<XOR7>(a); b += dpp<XOR7>(b);
  s0 = a * C2; s1 = b * C2;
}

__device__ __forceinline__ void pv(const Tile& v, f32x2 s, float M, float& l, float (&acc)[24]) {
  const float p0 = __builtin_amdgcn_exp2f(s[0] - M), p1 = __builtin_amdgcn_exp2f(s[1] - M);
  l += p0 + p1;
#pragma unroll
  for (int i = 0; i < 3; ++i)
#pragma unroll
    for (int j = 0; j < 4; ++j) {
      const unsigned u0 = v.r[i][j], u1 = v.r[3 + i][j];
      float lo = acc[i * 8 + 2 * j], hi = acc[i * 8 + 2 * j + 1];
      lo = fmaf(p0, __uint_as_float(u0 << 16), lo); hi = fmaf(p0, __uint_as_float(u0 & 0xffff0000u), hi);
      lo = fmaf(p1, __uint_as_float(u1 << 16), lo); hi = fmaf(p1, __uint_as_float(u1 & 0xffff0000u), hi);
      acc[i * 8 + 2 * j] = lo; acc[i * 8 + 2 * j + 1] = hi;
    }
}

__device__ __forceinline__ void wave_record(float* rec, int lane, float M, float l, float (&acc)[24]) {
  l += dpp<XOR8>(l); l = x16_32_sum(l);
  float x[12], y[6];
#pragma unroll
  for (int i = 0; i < 3; ++i)
#pragma unroll
    for (int e = 0; e < 4; ++e) {
      auto s = __builtin_amdgcn_permlane32_swap(__float_as_uint(acc[i * 8 + e]), __float_as_uint(acc[i * 8 + e + 4]), false, false);
      x[i * 4 + e] = __uint_as_float(s[0]) + __uint_as_float(s[1]);
    }
#pragma unroll
  for (int i = 0; i < 3; ++i)
#pragma unroll
    for (int e = 0; e < 2; ++e) {
      auto s = __builtin_amdgcn_permlane16_swap(__float_as_uint(x[i * 4 + e]), __float_as_uint(x[i * 4 + e + 2]), false, false);
      const float t = __uint_as_float(s[0]) + __uint_as_float(s[1]);
      y[i * 2 + e] = t + dpp<XOR8>(t);
    }
  if ((lane & 8) == 0) {
    float* p = rec + (lane & 7) * 8 + (lane >> 5) * 4 + ((lane >> 4) & 1) * 2;
#pragma unroll
    for (int i = 0; i < 3; ++i) *reinterpret_cast<f32x2*>(p + i * 64) = f32x2{y[i * 2], y[i * 2 + 1]};
    if (lane == 0) *reinterpret_cast<f32x2*>(rec + 192) = f32x2{M, l};
  }
}

__device__ __forceinline__ void part_merge(const float* lds, int lane, f32x4& o, float& MM, float& L) {
  const int dl = (lane < 48 ? lane : 0) * 4;
  f32x2 ml[WAVES]; f32x4 a[WAVES];
#pragma unroll
  for (int x = 0; x < WAVES; ++x) { ml[x] = *reinterpret_cast<const f32x2*>(lds + x * REC + 192); a[x] = *reinterpret_cast<const f32x4*>(lds + x * REC + dl); }
  SB();
  MM = fmaxf(fmaxf(ml[0][0], ml[1][0]), fmaxf(ml[2][0], ml[3][0]));
  L = 0.f;
  o = f32x4{0.f, 0.f, 0.f, 0.f};
#pragma unroll
  for (int x = 0; x < WAVES; ++x) {
    const float wt = __builtin_amdgcn_exp2f(ml[x][0] - MM);
    L = fmaf(wt, ml[x][1], L);
    o[0] = fmaf(wt, a[x][0], o[0]); o[1] = fmaf(wt, a[x][1], o[1]); o[2] = fmaf(wt, a[x][2], o[2]); o[3] = fmaf(wt, a[x][3], o[3]);
  }
}

#define PIN_MAX() asm volatile("" : "+v"(m) :: "memory")
#define PIN_ACC() asm volatile("" : "+v"(acc[0]), "+v"(acc[1]), "+v"(acc[2]), "+v"(acc[3]), "+v"(acc[4]), "+v"(acc[5]), "+v"(acc[6]), "+v"(acc[7]), \
  "+v"(acc[8]), "+v"(acc[9]), "+v"(acc[10]), "+v"(acc[11]), "+v"(acc[12]), "+v"(acc[13]), "+v"(acc[14]), "+v"(acc[15]), \
  "+v"(acc[16]), "+v"(acc[17]), "+v"(acc[18]), "+v"(acc[19]), "+v"(acc[20]), "+v"(acc[21]), "+v"(acc[22]), "+v"(acc[23]), "+v"(l) :: "memory")

template <bool MAIN>
__device__ __forceinline__ void stream_part(const char* kh, const char* vh, const uint16_t* qh, int light, int rot, int w, int lane, float* sc,
                                            float& M, float& l, float (&acc)[24], lu32* word, volatile unsigned* probe) {
  const int n = light ? NTL : NTH;
  const int voff = w * TILEB + (lane >> 3) * ROWB + (lane & 7) * 16;
  f32x2* const scw = reinterpret_cast<f32x2*>(sc) + (lane >> 3);
  u32x4 qf[3];
#pragma unroll
  for (int i = 0; i < 3; ++i) qf[i] = *reinterpret_cast<const u32x4*>(qh + i * 64 + (lane & 7) * 8);
  Tile t[RING];
  SB();
#pragma unroll
  for (int d = 0; d < RING; ++d) { load_tile(t[d], kh, vh, light, n, rot, d, voff); SB(); }
  if (MAIN) { if (w == 0) { unsigned tt = (unsigned)__builtin_amdgcn_s_memrealtime(); if (lane == 0) probe[0] = tt; } __builtin_amdgcn_s_barrier(); SB(); }
  unsigned tnext = MAIN ? clock16() - PACE : 0u;
  auto pace = [&](unsigned seen, int pos) {
    tnext += (pos < 2 * NTL + RING) ? PACE : PACE / 2;
    if ((int)(seen - tnext) < 0) { while ((int)(clock16() - tnext) < 0) __builtin_amdgcn_s_sleep(1); }
  };
  float m = -INFINITY;
  auto kstep = [&](Tile& tt, int pos) {
    float s0, s1;
    const unsigned now = MAIN ? clock16() : 0u;
    scores(tt, qf, s0, s1);
    m = fmaxf(m, fmaxf(s0, s1));
    if ((lane & 7) == 0) scw[pos * 8] = f32x2{s0, s1};
    PIN_MAX(); SB();
    if (MAIN) { pace(now, pos + RING); SB(); }
    load_tile(tt, kh, vh, light, n, rot, pos + RING, voff);
    SB();
  };
  auto vstep = [&](Tile& tt, int j) {
    const unsigned now = MAIN ? clock16() : 0u;
    pv(tt, scw[j * 8], M, l, acc);
    PIN_ACC(); SB();
    if (MAIN && n + j + RING < 2 * n) pace(now, n + j + RING);
    SB();
    load_tile(tt, kh, vh, light, n, rot, n + j + RING, voff);
    SB();
  };
  int p = 0;
  for (; p < n - MIX; p += RING) {
#pragma unroll
    for (int d = 0; d < RING; ++d) kstep(t[d], p + d);
  }
#pragma unroll
  for (int d = 0; d < MIX; ++d) kstep(t[d], p + d);
  M = fmaxf(m, dpp<XOR8>(m));
  M = fmaxf(x16_32_max(M), -3.0e38f);
  l = 0.f;
#pragma unroll
  for (int i = 0; i < 24; ++i) acc[i] = 0.f;
  SB();
#pragma unroll
  for (int d = MIX; d < (MIX ? RING : 0); ++d) vstep(t[d], d - MIX);
  for (int j = MIX ? RING - MIX : 0; j < n; j += RING) {
    if (MAIN && w == 0 && j + NEAR_END >= n && j + NEAR_END < n + RING) {
      if (lane == 0) __hip_atomic_fetch_or(word, L_NEAR, __ATOMIC_RELAXED, __HIP_MEMORY_SCOPE_WORKGROUP);
      SB();
    }
#pragma unroll
    for (int d = 0; d < RING; ++d) vstep(t[d], j + d);
  }
}

__global__ void __launch_bounds__((WAVES + 1) * 64, 2)
attn_decode(const uint16_t* __restrict__ q, const uint16_t* __restrict__ k, const uint16_t* __restrict__ v,
            __hip_bfloat16* out, char* ws) {
  __shared__ __attribute__((aligned(16))) float lds[WAVES * REC + 4 + WAVES * SCW + 8];
  unsigned long long t_top;
  asm volatile("s_memrealtime %0\n\ts_waitcnt lgkmcnt(0)" : "=s"(t_top) :: "memory");
  __builtin_amdgcn_sched_barrier(0);
  lu32* const arrived = (lu32*)(lds + WAVES * REC);
  float* const scores_lds = lds + WAVES * REC + 4;
  const int wg = blockIdx.x, head = wg >> 1;
  const int tid = threadIdx.x, lane = tid & 63, wave = __builtin_amdgcn_readfirstlane(tid >> 6);
  const int light = __builtin_amdgcn_s_getreg(20 | (3 << 11)) & 1;
  const char* kh = reinterpret_cast<const char*>(k) + (size_t)head * HEADB;
  const char* vh = reinterpret_cast<const char*>(v) + (size_t)head * HEADB;
  const uint16_t* qh = q + head * D;
  const int rot = (head & 3) * 8;
  float m, l, acc[24];

  if (__builtin_expect(wave < WAVES, 1)) {
    stream_part<true>(kh, vh, qh, light, rot, wave, lane, scores_lds + wave * SCW, m, l, acc, arrived, (volatile unsigned*)(lds + WAVES * REC + 4 + WAVES * SCW));
    wave_record(lds + wave * REC, lane, m, l, acc);
    asm volatile("s_waitcnt lgkmcnt(0)" ::: "memory");
    if (lane == 0) __hip_atomic_fetch_add(arrived, 1u, __ATOMIC_RELAXED, __HIP_MEMORY_SCOPE_WORKGROUP);
    return;
  }

  if (lane == 0) __hip_atomic_store(arrived, 0u, __ATOMIC_RELAXED, __HIP_MEMORY_SCOPE_WORKGROUP);
  asm volatile("s_waitcnt lgkmcnt(0)" ::: "memory");
  SB();
  __builtin_amdgcn_s_barrier();
  SB();
  gu32* const cnt = (gu32*)(ws + WS_CNT_OFF + head * 256);
  const __amdgpu_buffer_rsrc_t mybox = __builtin_amdgcn_make_buffer_rsrc((void*)(ws + (size_t)wg * WS_INBOX_B), 0, WS_INBOX_B, 0x00020000);
  const int boff = lane <= 48 ? lane * 16 : 0x7ff0;
  __builtin_amdgcn_raw_buffer_store_b128(u32x4{0u, 0u, 0u, 0u}, mybox, boff, 0, AUX_SC1);
  __builtin_amdgcn_raw_buffer_store_b128(u32x4{0u, 0u, 0u, 0u}, mybox, boff + 1024, 0, AUX_SC1);
  asm volatile("s_waitcnt vmcnt(0)" ::: "memory");
  unsigned tick = 0;
  if (lane == 0) tick = add_ret(cnt, P_START);
  tick = __builtin_amdgcn_readfirstlane(tick) + P_START;
  bool known = false, have = false;
  u32x4 r0 = {0, 0, 0, 0}, r1 = {0, 0, 0, 0};
  auto meet = [&]() {
    const unsigned d = peek(cnt) - tick;
    if (d & P_START) {
      if (lane == 0) add_ret(cnt, P_ACK);
      asm volatile("s_waitcnt vmcnt(0)" ::: "memory");
    }
    known = (d & (P_START | P_ACK)) != 0;
  };
  auto look = [&]() {
    asm volatile("" ::: "memory");
    r0 = __builtin_amdgcn_raw_buffer_load_b128(mybox, boff, 0, AUX_SC1);
    r1 = __builtin_amdgcn_raw_buffer_load_b128(mybox, boff + 1024, 0, AUX_SC1);
    const bool ok = lane > 48 || (((r0[1] ^ TAG) | (r0[3] ^ TAG) | (r1[1] ^ TAG) | (r1[3] ^ TAG)) >> 1) == 0;
    have = __all(ok);
  };
  while (!(lds_peek(arrived) & L_NEAR)) {
    if (known) __builtin_amdgcn_s_sleep(32);
    else { __builtin_amdgcn_s_sleep(127); meet(); }
  }
  for (;;) {
    look();
    if (have || (lds_peek(arrived) & L_COUNT) == WAVES) break;
    if (!known) meet();
    __builtin_amdgcn_s_sleep(4);
  }
  while ((lds_peek(arrived) & L_COUNT) != WAVES) __builtin_amdgcn_s_sleep(1);
  asm volatile("" ::: "memory");

  f32x4 o; float MM, L;
  part_merge(lds, lane, o, MM, L);

  bool later = false;
  if (!have) {
    for (unsigned spins = 0; !known && spins < SPIN_CAP; ++spins) { meet(); __builtin_amdgcn_s_sleep(4); }
    if (known) {
      const __amdgpu_buffer_rsrc_t peer = __builtin_amdgcn_make_buffer_rsrc((void*)(ws + (size_t)(wg ^ 1) * WS_INBOX_B), 0, WS_INBOX_B, 0x00020000);
      const unsigned tag = TAG | (unsigned)light;
      const float v0 = lane < 48 ? o[0] : MM, v1 = lane < 48 ? o[1] : L;
      __builtin_amdgcn_raw_buffer_store_b128(u32x4{__float_as_uint(v0), tag, __float_as_uint(v1), tag}, peer, boff, 0, AUX_SC1);
      __builtin_amdgcn_raw_buffer_store_b128(u32x4{__float_as_uint(o[2]), tag, __float_as_uint(o[3]), tag}, peer, boff + 1024, 0, AUX_SC1);
      unsigned e = 0;
      if (lane == 0) e = add_ret(cnt, P_FIN);
      look();
      if (!have) {
        later = ((__builtin_amdgcn_readfirstlane(e) - tick) & P_FIN) == 0;
        for (unsigned spins = 0; !later && !have && spins < SPIN_CAP; ++spins) look();
      }
    }
  }
  if (!later) {
    f32x4 p = {__uint_as_float(r0[0]), __uint_as_float(r0[2]), __uint_as_float(r1[0]), __uint_as_float(r1[2])};
    float pM = __uint_as_float(__builtin_amdgcn_readlane(r0[0], 48)), pL = __uint_as_float(__builtin_amdgcn_readlane(r0[2], 48));
    if (!have || (int)(__builtin_amdgcn_readlane(r0[1], 48) & 1) == light) {
      for (int w = 0; w < WAVES; ++w) {
        stream_part<false>(kh, vh, qh, light ^ 1, rot, w, lane, scores_lds + w * SCW, m, l, acc, arrived, (volatile unsigned*)(lds + WAVES * REC + 4 + WAVES * SCW));
        wave_record(lds + w * REC, lane, m, l, acc);
      }
      asm volatile("s_waitcnt lgkmcnt(0)" ::: "memory");
      part_merge(lds, lane, p, pM, pL);
    }
    const f32x4 oh = light ? p : o, ol = light ? o : p;
    const float Mh = light ? pM : MM, Ml = light ? MM : pM, Lh = light ? pL : L, Ll = light ? L : pL;
    const float Mx = fmaxf(Mh, Ml);
    const float wh = __builtin_amdgcn_exp2f(Mh - Mx), wl = __builtin_amdgcn_exp2f(Ml - Mx);
    const float inv = __builtin_amdgcn_rcpf(fmaf(wl, Ll, wh * Lh));
    const float a = wh * inv, b = wl * inv;
    if (lane < 48) {
      const bf16x2 x = {(__bf16)fmaf(b, ol[0], a * oh[0]), (__bf16)fmaf(b, ol[1], a * oh[1])};
      const bf16x2 y = {(__bf16)fmaf(b, ol[2], a * oh[2]), (__bf16)fmaf(b, ol[3], a * oh[3])};
      *reinterpret_cast<u32x2*>(reinterpret_cast<char*>(out) + (size_t)head * ROWB + lane * 8) =
          u32x2{__builtin_bit_cast(unsigned, x), __builtin_bit_cast(unsigned, y)};
    }
  }
  { const unsigned d = *(volatile unsigned*)(lds + WAVES * REC + 4 + WAVES * SCW) - (unsigned)t_top;
    const unsigned long long hold = 8000ull + 0ull * __builtin_amdgcn_readfirstlane(d);
    while (__builtin_amdgcn_s_memrealtime() - t_top < hold) __builtin_amdgcn_s_sleep(1); }
}

extern "C" void kernel_launch(void* const* d_in, const int* in_sizes, int n_in,
                              void* d_out, int out_size, void* d_ws, size_t ws_size,
                              hipStream_t stream) {
  const uint16_t* q = (const uint16_t*)d_in[0];
  const uint16_t* k = (const uint16_t*)d_in[1];
  const uint16_t* v = (const uint16_t*)d_in[2];
  attn_decode<<<NH * 2, (WAVES + 1) * 64, 0, stream>>>(q, k, v, (__hip_bfloat16*)d_out, (char*)d_ws);
}
```

```cpp
#include <hip/hip_runtime.h>
#include <hip/hip_bf16.h>
#include <stdint.h>
#pragma clang fp contract(off)

using u32x4 = __attribute__((ext_vector_type(4))) unsigned;
using u32x2 = __attribute__((ext_vector_type(2))) unsigned;
using f32x4 = __attribute__((ext_vector_type(4))) float;
using f32x2 = __attribute__((ext_vector_type(2))) float;
using bf16x2 = __attribute__((ext_vector_type(2))) __bf16;
typedef __attribute__((address_space(1))) unsigned gu32;
typedef __attribute__((address_space(3))) unsigned lu32;

constexpr int NH = 128, SKV = 4096, D = 192, ROWB = D * 2;
constexpr int HEADB = SKV * ROWB;
constexpr int WAVES = 4;
constexpr int TILE = 16;
constexpr int TILEB = TILE * ROWB;
constexpr int STEPB = WAVES * TILEB;
constexpr int NSTEP = HEADB / STEPB;
constexpr int NTH = 33, NTL = NSTEP - NTH;
constexpr int RING = 2;
constexpr int MIX = NTH % RING;
constexpr int NEAR_END = 8;
constexpr unsigned PACE = 1350;
constexpr int AUX_NT = 2, AUX_SC1 = 16;
static_assert(NTL % RING == MIX && (MIX == 0 || 2 * MIX == RING) && NTH >= NTL && NTL > 24 && NEAR_END < NTL - RING, "schedule");
constexpr float C2 = 0.07216878364870322f * 1.4426950408889634f;
constexpr int REC = 200;
constexpr int SCW = NTH * TILE;
constexpr unsigned L_NEAR = 0x100u, L_COUNT = 0xffu;
constexpr int WS_INBOX_B = 2048, WS_CNT_OFF = 1 << 20;
constexpr unsigned P_START = 1u, P_ACK = 4u, P_FIN = 16u;
constexpr unsigned TAG = 0x5EED0000u;
constexpr unsigned SPIN_CAP = 1u << 20;

#define SB() __builtin_amdgcn_sched_barrier(0)

__device__ __forceinline__ float dot2(unsigned a, unsigned b, float c) {
  return __builtin_amdgcn_fdot2_f32_bf16(__builtin_bit_cast(bf16x2, a), __builtin_bit_cast(bf16x2, b), c, false);
}
template <int CTRL> __device__ __forceinline__ float dpp(float x) {
  return __builtin_bit_cast(float, __builtin_amdgcn_mov_dpp(__builtin_bit_cast(int, x), CTRL, 0xf, 0xf, true));
}
constexpr int XOR1 = 0xB1, XOR2 = 0x4E, XOR7 = 0x141, XOR8 = 0x128;
__device__ __forceinline__ float x16_32_sum(float x) {
  auto s = __builtin_amdgcn_permlane16_swap(__float_as_uint(x), __float_as_uint(x), false, false);
  x = __uint_as_float(s[0]) + __uint_as_float(s[1]);
  auto t = __builtin_amdgcn_permlane32_swap(__float_as_uint(x), __float_as_uint(x), false, false);
  return __uint_as_float(t[0]) + __uint_as_float(t[1]);
}
__device__ __forceinline__ float x16_32_max(float x) {
  auto s = __builtin_amdgcn_permlane16_swap(__float_as_uint(x), __float_as_uint(x), false, false);
  x = fmaxf(__uint_as_float(s[0]), __uint_as_float(s[1]));
  auto t = __builtin_amdgcn_permlane32_swap(__float_as_uint(x), __float_as_uint(x), false, false);
  return fmaxf(__uint_as_float(t[0]), __uint_as_float(t[1]));
}
__device__ __forceinline__ unsigned add_ret(gu32* p, unsigned x) {
  int z = 0; asm volatile("" : "+v"(z));
  return __hip_atomic_fetch_add(p + z, x, __ATOMIC_RELAXED, __HIP_MEMORY_SCOPE_AGENT);
}
__device__ __forceinline__ unsigned peek(gu32* p) {
  return __builtin_amdgcn_readfirstlane(__hip_atomic_load(p, __ATOMIC_RELAXED, __HIP_MEMORY_SCOPE_AGENT));
}
__device__ __forceinline__ unsigned lds_peek(lu32* p) {
  return __builtin_amdgcn_readfirstlane(__hip_atomic_load(p, __ATOMIC_RELAXED, __HIP_MEMORY_SCOPE_WORKGROUP));
}
__device__ __forceinline__ unsigned clock16() { return (unsigned)__builtin_amdgcn_s_memrealtime() << 4; }

struct Tile { u32x4 r[6]; };

__device__ __forceinline__ void load_tile(Tile& t, const char* kh, const char* vh, int light, int n, int rot, int pos, int voff) {
  const bool isv = pos >= n;
  int i = (isv ? pos - n : pos) + rot;
  i = i >= n ? i - n : i;
  const int step = i + min(i, NTL) + light;
  const __amdgpu_buffer_rsrc_t rs = __builtin_amdgcn_make_buffer_rsrc((void*)(isv ? vh : kh), 0, pos < 2 * n ? HEADB : 0, 0x00020000);
  const int so = step * STEPB;
  t.r[0] = __builtin_amdgcn_raw_buffer_load_b128(rs, voff, so, AUX_NT);
  t.r[1] = __builtin_amdgcn_raw_buffer_load_b128(rs, voff + 128, so, AUX_NT);
  t.r[2] = __builtin_amdgcn_raw_buffer_load_b128(rs, voff + 256, so, AUX_NT);
  t.r[3] = __builtin_amdgcn_raw_buffer_load_b128(rs, voff + 8 * ROWB, so, AUX_NT);
  t.r[4] = __builtin_amdgcn_raw_buffer_load_b128(rs, voff + 8 * ROWB + 128, so, AUX_NT);
  t.r[5] = __builtin_amdgcn_raw_buffer_load_b128(rs, voff + 8 * ROWB + 256, so, AUX_NT);
}

__device__ __forceinline__ void scores(const Tile& k, const u32x4 (&q)[3], float& s0, float& s1) {
  float a = 0.f, b = 0.f;
#pragma unroll
  for (int i = 0; i < 3; ++i)
#pragma unroll
    for (int j = 0; j < 4; ++j) { a = dot2(k.r[i][j], q[i][j], a); b = dot2(k.r[3 + i][j], q[i][j], b); }
  a += dpp<XOR1>(a); b += dpp<XOR1>(b);
  a += dpp<XOR2>(a); b += dpp<XOR2>(b);
  a += dpp<XOR7>(a); b += dpp<XOR7>(b);
  s0 = a * C2; s1 = b * C2;
}

__device__ __forceinline__ void pv(const Tile& v, f32x2 s, float M, float& l, float (&acc)[24]) {
  const float p0 = __builtin_amdgcn_exp2f(s[0] - M), p1 = __builtin_amdgcn_exp2f(s[1] - M);
  l += p0 + p1;
#pragma unroll
  for (int i = 0; i < 3; ++i)
#pragma unroll
    for (int j = 0; j < 4; ++j) {
      const unsigned u0 = v.r[i][j], u1 = v.r[3 + i][j];
      float lo = acc[i * 8 + 2 * j], hi = acc[i * 8 + 2 * j + 1];
      lo = fmaf(p0, __uint_as_float(u0 << 16), lo); hi = fmaf(p0, __uint_as_float(u0 & 0xffff0000u), hi);
      lo = fmaf(p1, __uint_as_float(u1 << 16), lo); hi = fmaf(p1, __uint_as_float(u1 & 0xffff0000u), hi);
      acc[i * 8 + 2 * j] = lo; acc[i * 8 + 2 * j + 1] = hi;
    }
}

__device__ __forceinline__ void wave_record(float* rec, int lane, float M, float l, float (&acc)[24]) {
  l += dpp<XOR8>(l); l = x16_32_sum(l);
  float x[12], y[6];
#pragma unroll
  for (int i = 0; i < 3; ++i)
#pragma unroll
    for (int e = 0; e < 4; ++e) {
      auto s = __builtin_amdgcn_permlane32_swap(__float_as_uint(acc[i * 8 + e]), __float_as_uint(acc[i * 8 + e + 4]), false, false);
      x[i * 4 + e] = __uint_as_float(s[0]) + __uint_as_float(s[1]);
    }
#pragma unroll
  for (int i = 0; i < 3; ++i)
#pragma unroll
    for (int e = 0; e < 2; ++e) {
      auto s = __builtin_amdgcn_permlane16_swap(__float_as_uint(x[i * 4 + e]), __float_as_uint(x[i * 4 + e + 2]), false, false);
      const float t = __uint_as_float(s[0]) + __uint_as_float(s[1]);
      y[i * 2 + e] = t + dpp<XOR8>(t);
    }
  if ((lane & 8) == 0) {
    float* p = rec + (lane & 7) * 8 + (lane >> 5) * 4 + ((lane >> 4) & 1) * 2;
#pragma unroll
    for (int i = 0; i < 3; ++i) *reinterpret_cast<f32x2*>(p + i * 64) = f32x2{y[i * 2], y[i * 2 + 1]};
    if (lane == 0) *reinterpret_cast<f32x2*>(rec + 192) = f32x2{M, l};
  }
}

__device__ __forceinline__ void part_merge(const float* lds, int lane, f32x4& o, float& MM, float& L) {
  const int dl = (lane < 48 ? lane : 0) * 4;
  f32x2 ml[WAVES]; f32x4 a[WAVES];
#pragma unroll
  for (int x = 0; x < WAVES; ++x) { ml[x] = *reinterpret_cast<const f32x2*>(lds + x * REC + 192); a[x] = *reinterpret_cast<const f32x4*>(lds + x * REC + dl); }
  SB();
  MM = fmaxf(fmaxf(ml[0][0], ml[1][0]), fmaxf(ml[2][0], ml[3][0]));
  L = 0.f;
  o = f32x4{0.f, 0.f, 0.f, 0.f};
#pragma unroll
  for (int x = 0; x < WAVES; ++x) {
    const float wt = __builtin_amdgcn_exp2f(ml[x][0] - MM);
    L = fmaf(wt, ml[x][1], L);
    o[0] = fmaf(wt, a[x][0], o[0]); o[1] = fmaf(wt, a[x][1], o[1]); o[2] = fmaf(wt, a[x][2], o[2]); o[3] = fmaf(wt, a[x][3], o[3]);
  }
}

#define PIN_MAX() asm volatile("" : "+v"(m) :: "memory")
#define PIN_ACC() asm volatile("" : "+v"(acc[0]), "+v"(acc[1]), "+v"(acc[2]), "+v"(acc[3]), "+v"(acc[4]), "+v"(acc[5]), "+v"(acc[6]), "+v"(acc[7]), \
  "+v"(acc[8]), "+v"(acc[9]), "+v"(acc[10]), "+v"(acc[11]), "+v"(acc[12]), "+v"(acc[13]), "+v"(acc[14]), "+v"(acc[15]), \
  "+v"(acc[16]), "+v"(acc[17]), "+v"(acc[18]), "+v"(acc[19]), "+v"(acc[20]), "+v"(acc[21]), "+v"(acc[22]), "+v"(acc[23]), "+v"(l) :: "memory")

template <bool MAIN>
__device__ __forceinline__ void stream_part(const char* kh, const char* vh, const uint16_t* qh, int light, int rot, int w, int lane, float* sc,
                                            float& M, float& l, float (&acc)[24], lu32* word) {
  const int n = light ? NTL : NTH;
  const int voff = w * TILEB + (lane >> 3) * ROWB + (lane & 7) * 16;
  f32x2* const scw = reinterpret_cast<f32x2*>(sc) + (lane >> 3);
  u32x4 qf[3];
#pragma unroll
  for (int i = 0; i < 3; ++i) qf[i] = *reinterpret_cast<const u32x4*>(qh + i * 64 + (lane & 7) * 8);
  Tile t[RING];
  SB();
#pragma unroll
  for (int d = 0; d < RING; ++d) { load_tile(t[d], kh, vh, light, n, rot, d, voff); SB(); }
  if (MAIN) { __builtin_amdgcn_s_barrier(); SB(); }
  unsigned tnext = MAIN ? clock16() - PACE : 0u;
  auto pace = [&](unsigned seen, int pos) {
    tnext += (pos < 2 * NTL + RING) ? PACE : PACE / 2;
    if ((int)(seen - tnext) < 0) { while ((int)(clock16() - tnext) < 0) __builtin_amdgcn_s_sleep(1); }
  };
  float m = -INFINITY;
  auto kstep = [&](Tile& tt, int pos) {
    float s0, s1;
    const unsigned now = MAIN ? clock16() : 0u;
    scores(tt, qf, s0, s1);
    m = fmaxf(m, fmaxf(s0, s1));
    if ((lane & 7) == 0) scw[pos * 8] = f32x2{s0, s1};
    PIN_MAX(); SB();
    if (MAIN) { pace(now, pos + RING); SB(); }
    load_tile(tt, kh, vh, light, n, rot, pos + RING, voff);
    SB();
  };
  auto vstep = [&](Tile& tt, int j) {
    const unsigned now = MAIN ? clock16() : 0u;
    pv(tt, scw[j * 8], M, l, acc);
    PIN_ACC(); SB();
    if (MAIN && n + j + RING < 2 * n) pace(now, n + j + RING);
    SB();
    load_tile(tt, kh, vh, light, n, rot, n + j + RING, voff);
    SB();
  };
  int p = 0;
  for (; p < n - MIX; p += RING) {
#pragma unroll
    for (int d = 0; d < RING; ++d) kstep(t[d], p + d);
  }
#pragma unroll
  for (int d = 0; d < MIX; ++d) kstep(t[d], p + d);
  M = fmaxf(m, dpp<XOR8>(m));
  M = fmaxf(x16_32_max(M), -3.0e38f);
  l = 0.f;
#pragma unroll
  for (int i = 0; i < 24; ++i) acc[i] = 0.f;
  SB();
#pragma unroll
  for (int d = MIX; d < (MIX ? RING : 0); ++d) vstep(t[d], d - MIX);
  for (int j = MIX ? RING - MIX : 0; j < n; j += RING) {
    if (MAIN && w == 0 && j + NEAR_END >= n && j + NEAR_END < n + RING) {
      if (lane == 0) __hip_atomic_fetch_or(word, L_NEAR, __ATOMIC_RELAXED, __HIP_MEMORY_SCOPE_WORKGROUP);
      SB();
    }
#pragma unroll
    for (int d = 0; d < RING; ++d) vstep(t[d], j + d);
  }
}

__global__ void __launch_bounds__((WAVES + 1) * 64, 2)
attn_decode(const uint16_t* __restrict__ q, const uint16_t* __restrict__ k, const uint16_t* __restrict__ v,
            __hip_bfloat16* out, char* ws) {
  __shared__ __attribute__((aligned(16))) float lds[WAVES * REC + 4 + WAVES * SCW];
  lu32* const arrived = (lu32*)(lds + WAVES * REC);
  float* const scores_lds = lds + WAVES * REC + 4;
  const int wg = blockIdx.x, head = wg >> 1;
  const int tid = threadIdx.x, lane = tid & 63, wave = __builtin_amdgcn_readfirstlane(tid >> 6);
  const int light = __builtin_amdgcn_s_getreg(20 | (3 << 11)) & 1;
  const char* kh = reinterpret_cast<const char*>(k) + (size_t)head * HEADB;
  const char* vh = reinterpret_cast<const char*>(v) + (size_t)head * HEADB;
  const uint16_t* qh = q + head * D;
  const int rot = (head & 3) * 8;
  float m, l, acc[24];

  if (__builtin_expect(wave < WAVES, 1)) {
    stream_part<true>(kh, vh, qh, light, rot, wave, lane, scores_lds + wave * SCW, m, l, acc, arrived);
    wave_record(lds + wave * REC, lane, m, l, acc);
    asm volatile("s_waitcnt lgkmcnt(0)" ::: "memory");
    if (lane == 0) __hip_atomic_fetch_add(arrived, 1u, __ATOMIC_RELAXED, __HIP_MEMORY_SCOPE_WORKGROUP);
    return;
  }

  if (lane == 0) __hip_atomic_store(arrived, 0u, __ATOMIC_RELAXED, __HIP_MEMORY_SCOPE_WORKGROUP);
  asm volatile("s_waitcnt lgkmcnt(0)" ::: "memory");
  SB();
  __builtin_amdgcn_s_barrier();
  SB();
  gu32* const cnt = (gu32*)(ws + WS_CNT_OFF + head * 256);
  const __amdgpu_buffer_rsrc_t mybox = __builtin_amdgcn_make_buffer_rsrc((void*)(ws + (size_t)wg * WS_INBOX_B), 0, WS_INBOX_B, 0x00020000);
  const int boff = lane <= 48 ? lane * 16 : 0x7ff0;
  __builtin_amdgcn_raw_buffer_store_b128(u32x4{0u, 0u, 0u, 0u}, mybox, boff, 0, AUX_SC1);
  __builtin_amdgcn_raw_buffer_store_b128(u32x4{0u, 0u, 0u, 0u}, mybox, boff + 1024, 0, AUX_SC1);
  asm volatile("s_waitcnt vmcnt(0)" ::: "memory");
  unsigned tick = 0;
  if (lane == 0) tick = add_ret(cnt, P_START);
  tick = __builtin_amdgcn_readfirstlane(tick) + P_START;
  bool known = false, have = false;
  u32x4 r0 = {0, 0, 0, 0}, r1 = {0, 0, 0, 0};
  auto meet = [&]() {
    const unsigned d = peek(cnt) - tick;
    if (d & P_START) {
      if (lane == 0) add_ret(cnt, P_ACK);
      asm volatile("s_waitcnt vmcnt(0)" ::: "memory");
    }
    known = (d & (P_START | P_ACK)) != 0;
  };
  auto look = [&]() {
    asm volatile("" ::: "memory");
    r0 = __builtin_amdgcn_raw_buffer_load_b128(mybox, boff, 0, AUX_SC1);
    r1 = __builtin_amdgcn_raw_buffer_load_b128(mybox, boff + 1024, 0, AUX_SC1);
    const bool ok = lane > 48 || (((r0[1] ^ TAG) | (r0[3] ^ TAG) | (r1[1] ^ TAG) | (r1[3] ^ TAG)) >> 1) == 0;
    have = __all(ok);
  };
  while (!(lds_peek(arrived) & L_NEAR)) {
    if (known) __builtin_amdgcn_s_sleep(32);
    else { __builtin_amdgcn_s_sleep(127); meet(); }
  }
  for (;;) {
    look();
    if (have || (lds_peek(arrived) & L_COUNT) == WAVES) break;
    if (!known) meet();
    __builtin_amdgcn_s_sleep(4);
  }
  while ((lds_peek(arrived) & L_COUNT) != WAVES) __builtin_amdgcn_s_sleep(1);
  asm volatile("" ::: "memory");

  f32x4 o; float MM, L;
  part_merge(lds, lane, o, MM, L);

  bool later = false;
  if (!have) {
    for (unsigned spins = 0; !known && spins < SPIN_CAP; ++spins) { meet(); __builtin_amdgcn_s_sleep(4); }
    if (known) {
      const __amdgpu_buffer_rsrc_t peer = __builtin_amdgcn_make_buffer_rsrc((void*)(ws + (size_t)(wg ^ 1) * WS_INBOX_B), 0, WS_INBOX_B, 0x00020000);
      const unsigned tag = TAG | (unsigned)light;
      const float v0 = lane < 48 ? o[0] : MM, v1 = lane < 48 ? o[1] : L;
      __builtin_amdgcn_raw_buffer_store_b128(u32x4{__float_as_uint(v0), tag, __float_as_uint(v1), tag}, peer, boff, 0, AUX_SC1);
      __builtin_amdgcn_raw_buffer_store_b128(u32x4{__float_as_uint(o[2]), tag, __float_as_uint(o[3]), tag}, peer, boff + 1024, 0, AUX_SC1);
      unsigned e = 0;
      if (lane == 0) e = add_ret(cnt, P_FIN);
      look();
      if (!have) {
        later = ((__builtin_amdgcn_readfirstlane(e) - tick) & P_FIN) == 0;
        for (unsigned spins = 0; !later && !have && spins < SPIN_CAP; ++spins) look();
      }
    }
  }
  if (!later) {
    f32x4 p = {__uint_as_float(r0[0]), __uint_as_float(r0[2]), __uint_as_float(r1[0]), __uint_as_float(r1[2])};
    float pM = __uint_as_float(__builtin_amdgcn_readlane(r0[0], 48)), pL = __uint_as_float(__builtin_amdgcn_readlane(r0[2], 48));
    if (!have || (int)(__builtin_amdgcn_readlane(r0[1], 48) & 1) == light) {
      for (int w = 0; w < WAVES; ++w) {
        stream_part<false>(kh, vh, qh, light ^ 1, rot, w, lane, scores_lds + w * SCW, m, l, acc, arrived);
        wave_record(lds + w * REC, lane, m, l, acc);
      }
      asm volatile("s_waitcnt lgkmcnt(0)" ::: "memory");
      part_merge(lds, lane, p, pM, pL);
    }
    const f32x4 oh = light ? p : o, ol = light ? o : p;
    const float Mh = light ? pM : MM, Ml = light ? MM : pM, Lh = light ? pL : L, Ll = light ? L : pL;
    const float Mx = fmaxf(Mh, Ml);
    const float wh = __builtin_amdgcn_exp2f(Mh - Mx), wl = __builtin_amdgcn_exp2f(Ml - Mx);
    const float inv = __builtin_amdgcn_rcpf(fmaf(wl, Ll, wh * Lh));
    const float a = wh * inv, b = wl * inv;
    if (lane < 48) {
      const bf16x2 x = {(__bf16)fmaf(b, ol[0], a * oh[0]), (__bf16)fmaf(b, ol[1], a * oh[1])};
      const bf16x2 y = {(__bf16)fmaf(b, ol[2], a * oh[2]), (__bf16)fmaf(b, ol[3], a * oh[3])};
      *reinterpret_cast<u32x2*>(reinterpret_cast<char*>(out) + (size_t)head * ROWB + lane * 8) =
          u32x2{__builtin_bit_cast(unsigned, x), __builtin_bit_cast(unsigned, y)};
    }
  }
}

extern "C" void kernel_launch(void* const* d_in, const int* in_sizes, int n_in,
                              void* d_out, int out_size, void* d_ws, size_t ws_size,
                              hipStream_t stream) {
  const uint16_t* q = (const uint16_t*)d_in[0];
  const uint16_t* k = (const uint16_t*)d_in[1];
  const uint16_t* v = (const uint16_t*)d_in[2];
  attn_decode<<<NH * 2, (WAVES + 1) * 64, 0, stream>>>(q, k, v, (__hip_bfloat16*)d_out, (char*)d_ws);
}
```
